```python
import torch

def _softmax(x, axis=-1):
    return torch.softmax(x, dim=axis)
def _gelu(x):
    return torch.nn.functional.gelu(x, approximate="tanh")
def _silu(x):
    return torch.nn.functional.silu(x)
def _sigmoid(x):
    return torch.sigmoid(x)
def _layernorm(x, axis=-1, eps=1e-5):
    m = x.mean(dim=axis, keepdim=True)
    v = x.var(dim=axis, keepdim=True, unbiased=False)
    return (x - m) / torch.sqrt(v + eps)
def _scatter_add(tgt, idx, src, axis=0):
    return tgt.clone().index_add_(axis, idx, src)
def _segment_sum(src, seg, n):
    out = torch.zeros((n,) + src.shape[1:], dtype=src.dtype)
    return out.index_add_(0, seg, src)
def _pad(x, pw):
    flat = tuple(v for pair in reversed(pw) for v in pair)
    return torch.nn.functional.pad(x, flat)

kernel_name = 'gated_mix__sliding_block_cached__dense_activation_squared_relu__decode'

def setup_inputs():

    _w = (lambda a: (lambda t: t.to(torch.bfloat16) if t.is_floating_point() else t)(torch.as_tensor(a)))
    def _A_setup():
        B, Sq, D = 1, 1, 448
        Hq, Hkv, Sk, Dh = 16, 8, 65536, 256
        assert Hq % Hkv == 0, (Hq, Hkv)
        return {
            "x": _w((lambda *s: torch.randn(*s, dtype=torch.bfloat16) * 0.05)(B, Sq, D)),
            "k_cache": _w((lambda *s: torch.randn(*s, dtype=torch.bfloat16) * 3.2)(B, Hkv, Sk, Dh)),
            "v_cache": _w((lambda *s: torch.randn(*s, dtype=torch.bfloat16) * 1.6)(B, Hkv, Sk, Dh)),
            "wq": _w((lambda *s: torch.randn(*s, dtype=torch.bfloat16) * 3.2)(D, Hq * Dh) * (D ** -0.5)),
            "wo": _w((lambda *s: torch.randn(*s, dtype=torch.bfloat16) * 1.6)(Hq * Dh, D) * ((Hq * Dh) ** -0.5)),
        }
    _A_in = _A_setup()

    def _B_setup():
        B, S, D, F = 1, 1, 448, 1792
        return {
            "x": _w((lambda *s: torch.randn(*s, dtype=torch.bfloat16) * 0.05)(B, S, D)),
            "w1": _w((lambda *s: torch.randn(*s, dtype=torch.bfloat16) * 0.1)(D, F)),
            "b1": _w((lambda *s: torch.randn(*s, dtype=torch.bfloat16) * 0.05)(F)),
            "w2": _w((lambda *s: torch.randn(*s, dtype=torch.bfloat16) * 0.05)(F, D)),
            "b2": _w((lambda *s: torch.randn(*s, dtype=torch.bfloat16) * 0.05)(D)),
        }
    _B_in = _B_setup()

    return {
        "A_x": _A_in["x"],
        "A_k_cache": _A_in["k_cache"],
        "A_v_cache": _A_in["v_cache"],
        "A_wq": _A_in["wq"],
        "A_wo": _A_in["wo"],
        "B_x": _B_in["x"],
        "B_w1": _B_in["w1"],
        "B_b1": _B_in["b1"],
        "B_w2": _B_in["w2"],
        "B_b2": _B_in["b2"],
        "gate_w": _w((lambda *s: torch.randn(*s, dtype=torch.bfloat16) * 0.05)(448) * 16.0),
    }

def reference(A_x, A_k_cache, A_v_cache, A_wq, A_wo, B_x, B_w1, B_b1, B_w2, B_b2, gate_w):
    def _A_ref(x, k_cache, v_cache, wq, wo):
        B, Sq, D = x.shape
        Hq, Dh = 16, 256
        Hkv, Sk = k_cache.shape[1], k_cache.shape[2]
        W = min(4096, max(256, Sk // 4))
        rep = Hq // Hkv
        q = (lambda x, *d: x.permute(*d))((x @ wq).reshape(B, Sq, Hq, Dh), 0, 2, 1, 3)
        k = (lambda x, n, axis: torch.repeat_interleave(x, n, dim=axis))(k_cache[:, :, Sk - W:, :], rep, axis=1)
        v = (lambda x, n, axis: torch.repeat_interleave(x, n, dim=axis))(v_cache[:, :, Sk - W:, :], rep, axis=1)
        scores = torch.einsum("bhqd,bhkd->bhqk", q, k) * (Dh ** -0.5)
        p = _softmax(scores, axis=-1)
        o = torch.einsum("bhqk,bhkd->bhqd", p, v)
        return (lambda x, *d: x.permute(*d))(o, 0, 2, 1, 3).reshape(B, Sq, Hq * Dh) @ wo

    def _B_ref(x, w1, b1, w2, b2):
        h = torch.einsum("bsd,df->bsf", x, w1) + b1
        zero = torch.zeros_like(h)
        r = torch.where(h > zero, h, zero)
        h = r * r
        out = torch.einsum("bsf,fd->bsd", h, w2) + b2
        return out

    x = A_x
    g = _sigmoid((x * gate_w).sum(-1, keepdims=True))
    a = _A_ref(x, A_k_cache, A_v_cache, A_wq, A_wo)
    b = _B_ref(x, B_w1, B_b1, B_w2, B_b2)
    return g * a + (1 - g) * b
```

```cpp
#include <hip/hip_runtime.h>
#include <hip/hip_bf16.h>
#include <stdint.h>

typedef unsigned short u16;
typedef unsigned u32x4 __attribute__((ext_vector_type(4)));
typedef __bf16 bf16x2 __attribute__((ext_vector_type(2)));

constexpr int D = 448, HQ = 16, HKV = 8, SK = 65536, DH = 256, W = 4096, F = 1792;
constexpr int NSPLIT = 32, KPS = W / NSPLIT;

__device__ __forceinline__ float bf2f(u16 v) { return __uint_as_float(((unsigned)v) << 16); }
__device__ __forceinline__ float rbf(float f) {
  unsigned u = __float_as_uint(f);
  u = (u + 0x7fffu + ((u >> 16) & 1u)) & 0xffff0000u;
  return __uint_as_float(u);
}
__device__ __forceinline__ float dot2(unsigned a, unsigned b, float acc) {
  return __builtin_amdgcn_fdot2_f32_bf16(__builtin_bit_cast(bf16x2, a), __builtin_bit_cast(bf16x2, b), acc, false);
}

__global__ void k1(const u16* x, const u16* wq, const u16* w1, const u16* b1, float* q, float* hsq) {
  __shared__ float red[4][64];
  __shared__ float xs[D];
  int tid = threadIdx.x, lane = tid & 63, wv = tid >> 6;
  for (int i = tid; i < D; i += 256) xs[i] = bf2f(x[i]);
  __syncthreads();
  int col = blockIdx.x * 64 + lane;
  const u16* wp; int ld;
  if (col < HQ * DH) { wp = wq + col; ld = HQ * DH; } else { wp = w1 + (col - HQ * DH); ld = F; }
  float acc = 0.f;
  for (int k = wv * 112; k < wv * 112 + 112; ++k) acc = fmaf(xs[k], bf2f(wp[(size_t)k * ld]), acc);
  red[wv][lane] = acc;
  __syncthreads();
  if (wv == 0) {
    float s = red[0][lane] + red[1][lane] + red[2][lane] + red[3][lane];
    if (col < HQ * DH) q[col] = rbf(s);
    else {
      int f = col - HQ * DH;
      float h = rbf(rbf(s) + bf2f(b1[f]));
      float r = h > 0.f ? h : 0.f;
      hsq[f] = rbf(r * r);
    }
  }
}

__global__ void k2(const u16* kc, const u16* vc, const float* q, float* po, float* pml) {
  __shared__ float sc[2][KPS];
  __shared__ float red[4][2][DH];
  __shared__ float ml[2][2];
  int tid = threadIdx.x, lane = tid & 63, wv = tid >> 6;
  int s = blockIdx.x, h = blockIdx.y;
  const u16* kb = kc + ((size_t)h * SK + (SK - W) + s * KPS) * DH;
  const u16* vb = vc + ((size_t)h * SK + (SK - W) + s * KPS) * DH;
  float q0[8], q1[8];
  for (int j = 0; j < 8; ++j) { q0[j] = q[(2 * h) * DH + (lane & 31) * 8 + j]; q1[j] = q[(2 * h + 1) * DH + (lane & 31) * 8 + j]; }
  for (int i = 0; i < 16; ++i) {
    int key = wv * 32 + i * 2 + (lane >> 5);
    u32x4 kv = *(const u32x4*)(kb + (size_t)key * DH + (lane & 31) * 8);
    float a0 = 0.f, a1 = 0.f;
    for (int j = 0; j < 4; ++j) {
      float lo = __uint_as_float(kv[j] << 16), hi = __uint_as_float(kv[j] & 0xffff0000u);
      a0 = fmaf(lo, q0[2 * j], a0); a0 = fmaf(hi, q0[2 * j + 1], a0);
      a1 = fmaf(lo, q1[2 * j], a1); a1 = fmaf(hi, q1[2 * j + 1], a1);
    }
    for (int m = 16; m >= 1; m >>= 1) { a0 += __shfl_xor(a0, m); a1 += __shfl_xor(a1, m); }
    if ((lane & 31) == 0) { sc[0][key] = a0 * 0.0625f; sc[1][key] = a1 * 0.0625f; }
  }
  __syncthreads();
  if (wv < 2) {
    float v0 = sc[wv][lane], v1 = sc[wv][lane + 64];
    float m = fmaxf(v0, v1);
    for (int o = 32; o >= 1; o >>= 1) m = fmaxf(m, __shfl_xor(m, o));
    float e0 = __expf(v0 - m), e1 = __expf(v1 - m);
    float l = e0 + e1;
    for (int o = 32; o >= 1; o >>= 1) l += __shfl_xor(l, o);
    sc[wv][lane] = e0; sc[wv][lane + 64] = e1;
    if (lane == 0) { ml[wv][0] = m; ml[wv][1] = l; }
  }
  __syncthreads();
  float o0[8] = {0}, o1[8] = {0};
  for (int i = 0; i < 16; ++i) {
    int key = wv * 32 + i * 2 + (lane >> 5);
    u32x4 vv = *(const u32x4*)(vb + (size_t)key * DH + (lane & 31) * 8);
    float p0 = sc[0][key], p1 = sc[1][key];
    for (int j = 0; j < 4; ++j) {
      float lo = __uint_as_float(vv[j] << 16), hi = __uint_as_float(vv[j] & 0xffff0000u);
      o0[2 * j] = fmaf(p0, lo, o0[2 * j]); o0[2 * j + 1] = fmaf(p0, hi, o0[2 * j + 1]);
      o1[2 * j] = fmaf(p1, lo, o1[2 * j]); o1[2 * j + 1] = fmaf(p1, hi, o1[2 * j + 1]);
    }
  }
  for (int j = 0; j < 8; ++j) { o0[j] += __shfl_xor(o0[j], 32); o1[j] += __shfl_xor(o1[j], 32); }
  if (lane < 32) for (int j = 0; j < 8; ++j) { red[wv][0][lane * 8 + j] = o0[j]; red[wv][1][lane * 8 + j] = o1[j]; }
  __syncthreads();
  for (int hh = 0; hh < 2; ++hh) {
    float v = red[0][hh][tid] + red[1][hh][tid] + red[2][hh][tid] + red[3][hh][tid];
    po[((size_t)(2 * h + hh) * NSPLIT + s) * DH + tid] = v;
  }
  if (tid < 2) { pml[((2 * h + tid) * NSPLIT + s) * 2] = ml[tid][0]; pml[((2 * h + tid) * NSPLIT + s) * 2 + 1] = ml[tid][1]; }
}

__global__ void k3(const float* po, const float* pml, const float* hsq, const u16* wo, const u16* w2, float* part) {
  __shared__ float o[256];
  int tid = threadIdx.x, b = blockIdx.x;
  const u16* wp;
  if (b < HQ) {
    float M = -INFINITY;
    for (int s = 0; s < NSPLIT; ++s) M = fmaxf(M, pml[(b * NSPLIT + s) * 2]);
    float L = 0.f, acc = 0.f;
    for (int s = 0; s < NSPLIT; ++s) {
      float w = __expf(pml[(b * NSPLIT + s) * 2] - M);
      L += w * pml[(b * NSPLIT + s) * 2 + 1];
      acc += w * po[((size_t)b * NSPLIT + s) * DH + tid];
    }
    o[tid] = rbf(acc / L);
    wp = wo + (size_t)b * 256 * D;
  } else {
    o[tid] = hsq[(b - HQ) * 256 + tid];
    wp = w2 + (size_t)(b - HQ) * 256 * D;
  }
  __syncthreads();
  if (tid < 224) {
    float a0 = 0.f, a1 = 0.f;
    for (int r = 0; r < 256; ++r) {
      unsigned w = *(const unsigned*)(wp + (size_t)r * D + tid * 2);
      a0 = fmaf(o[r], __uint_as_float(w << 16), a0);
      a1 = fmaf(o[r], __uint_as_float(w & 0xffff0000u), a1);
    }
    part[b * D + tid * 2] = a0; part[b * D + tid * 2 + 1] = a1;
  }
}

__global__ void k4(const float* part, const u16* x, const u16* gw, const u16* b2, u16* out) {
  __shared__ float red[8];
  int tid = threadIdx.x;
  float pr = rbf(bf2f(x[tid]) * bf2f(gw[tid]));
  for (int o = 32; o >= 1; o >>= 1) pr += __shfl_xor(pr, o);
  if ((tid & 63) == 0) red[tid >> 6] = pr;
  __syncthreads();
  float z = 0.f;
  for (int i = 0; i < 7; ++i) z += red[i];
  z = rbf(z);
  float g = rbf(1.f / (1.f + __expf(-z)));
  float a = 0.f, b = 0.f;
  for (int i = 0; i < HQ; ++i) a += part[i * D + tid];
  for (int i = 0; i < 7; ++i) b += part[(HQ + i) * D + tid];
  a = rbf(a);
  b = rbf(rbf(b) + bf2f(b2[tid]));
  float r = rbf(rbf(g * a) + rbf(rbf(1.f - g) * b));
  out[tid] = (u16)(__float_as_uint(r) >> 16);
}

extern "C" void kernel_launch(void* const* d_in, const int* in_sizes, int n_in, void* d_out, int out_size, void* d_ws,
                              size_t ws_size, hipStream_t stream) {
  const u16* x = (const u16*)d_in[0];
  const u16* kc = (const u16*)d_in[1];
  const u16* vc = (const u16*)d_in[2];
  const u16* wq = (const u16*)d_in[3];
  const u16* wo = (const u16*)d_in[4];
  const u16* w1 = (const u16*)d_in[6];
  const u16* b1 = (const u16*)d_in[7];
  const u16* w2 = (const u16*)d_in[8];
  const u16* b2 = (const u16*)d_in[9];
  const u16* gw = (const u16*)d_in[10];
  float* ws = (float*)d_ws;
  float* q = ws;
  float* hsq = ws + 4096;
  float* pml = ws + 8192;
  float* po = ws + 16384;
  float* part = ws + 16384 + 131072;
  k1<<<92, 256, 0, stream>>>(x, wq, w1, b1, q, hsq);
  k2<<<dim3(NSPLIT, HKV), 256, 0, stream>>>(kc, vc, q, po, pml);
  k3<<<23, 256, 0, stream>>>(po, pml, hsq, wo, w2, part);
  k4<<<1, 448, 0, stream>>>(part, x, gw, b2, (u16*)d_out);
}
```

```cpp
#define HD1 0
#define HD2 0
#include <hip/hip_runtime.h>
#include <hip/hip_bf16.h>
#include <stdint.h>

typedef unsigned long long u64;
typedef unsigned short u16;
typedef short s16x4 __attribute__((ext_vector_type(4)));
typedef __bf16 bf16x8 __attribute__((ext_vector_type(8)));
typedef float f32x4 __attribute__((ext_vector_type(4)));
typedef float f32x2 __attribute__((ext_vector_type(2)));
typedef unsigned u32x4 __attribute__((ext_vector_type(4)));
typedef unsigned u32x2 __attribute__((ext_vector_type(2)));
typedef unsigned u32x8 __attribute__((ext_vector_type(8)));
typedef unsigned u32x16 __attribute__((ext_vector_type(16)));
#define LAS __attribute__((address_space(3)))

constexpr int SK = 65536, WIN = 4096;
constexpr unsigned F_L = 0  , F_C = 32  , F_Q = 48  , F_R = 64  ;
constexpr unsigned GATE = 96  , MX = 128, SUMS = 160, WSC = 192  , QS = 512  , PS = 1792  ,
                   HS = 2432  , RED = 2560  ;
constexpr unsigned WQ_OFF = 4096, W1_OFF = 18432, WO_OFF = 4096, W2_OFF = 20480, K_OFF = 32768, V_OFF = 98304, STG = K_OFF;
constexpr int LDS_BYTES = 163840;
constexpr unsigned WS_EPOCH = 0, WS_ML = 8192  , WS_QG = 65536  , WS_HG = 131072  , WS_IN1 = 262144  ,
                   WS_IN2 = 524288  ;
constexpr unsigned WS_STAMP = 8u << 20;
constexpr unsigned SPIN_CAP = 1u << 16;
#define QD0 16
#define WK 2
#define WV 2
#define WV2 2
#define STR2(x) #x
#define STR(x) STR2(x)

#define WAITV(n) asm volatile("s_waitcnt vmcnt(" STR(n) ")" ::: "memory")
#define BARRIER() asm volatile("s_waitcnt lgkmcnt(0)\n\ts_barrier" ::: "memory")

#define ST(i)

__device__ __forceinline__ const unsigned char* uni(const void* p) { return (const unsigned char*)p; }
__device__ __forceinline__ const unsigned char* uptr(const void* p) {
  const u64 a = (u64)p;
  return (const unsigned char*)(((u64)(unsigned)__builtin_amdgcn_readfirstlane((unsigned)(a >> 32)) << 32) | (unsigned)__builtin_amdgcn_readfirstlane((unsigned)a));
}
#define DMA1(v0, sb, lds) \
  asm volatile("s_mov_b32 m0, %2\n\ts_nop 0\n\tglobal_load_lds_dwordx4 %0, %1 nt" ::"v"(v0), "s"(sb), "s"(lds) : "memory")
#define LD4(dst, voff, sb, mods) asm volatile("global_load_dwordx4 %0, %1, %2 " mods : "=v"(dst) : "v"(voff), "s"(sb) : "memory")
#define LD2(dst, voff, sb, mods) asm volatile("global_load_dwordx2 %0, %1, %2 " mods : "=v"(dst) : "v"(voff), "s"(sb) : "memory")
#define LDX4(dst, voff, sb, mods) asm volatile("global_load_dwordx4 %0, %1, %2 " mods : "+v"(dst) : "v"(voff), "s"(sb) : "memory")
#define LDX2(dst, voff, sb, mods) asm volatile("global_load_dwordx2 %0, %1, %2 " mods : "+v"(dst) : "v"(voff), "s"(sb) : "memory")
#define LDU(dst, voff, sb, mods) asm volatile("global_load_ushort %0, %1, %2 " mods : "=v"(dst) : "v"(voff), "s"(sb) : "memory")
#define ST4(voff, val, sb, mods) asm volatile("global_store_dwordx4 %0, %1, %2 " mods "\n\ts_nop 1" ::"v"(voff), "v"(val), "s"(sb) : "memory")
#define ST2(voff, val, sb, mods) asm volatile("global_store_dwordx2 %0, %1, %2 " mods ::"v"(voff), "v"(val), "s"(sb) : "memory")
#define ST1(voff, val, sb, mods) asm volatile("global_store_dword %0, %1, %2 " mods ::"v"(voff), "v"(val), "s"(sb) : "memory")
#define STH(voff, val, sb, mods) asm volatile("global_store_short %0, %1, %2 " mods ::"v"(voff), "v"(val), "s"(sb) : "memory")

template <int CTRL> __device__ __forceinline__ float dpp(float x) {
  return __builtin_bit_cast(float, __builtin_amdgcn_mov_dpp(__builtin_bit_cast(int, x), CTRL, 0xf, 0xf, true));
}
constexpr int ROR8 = 0x128, ROR4 = 0x124, ROR2 = 0x122, ROR1 = 0x121;
__device__ __forceinline__ float row_max(float x) { x = fmaxf(x, dpp<ROR8>(x)); x = fmaxf(x, dpp<ROR4>(x)); x = fmaxf(x, dpp<ROR2>(x)); return fmaxf(x, dpp<ROR1>(x)); }
__device__ __forceinline__ float row_sum(float x) { x += dpp<ROR8>(x); x += dpp<ROR4>(x); x += dpp<ROR2>(x); return x + dpp<ROR1>(x); }
__device__ __forceinline__ float fold32(float a, float b) {
  auto s = __builtin_amdgcn_permlane32_swap(__float_as_uint(a), __float_as_uint(b), false, false);
  return __uint_as_float(s[0]) + __uint_as_float(s[1]);
}
__device__ __forceinline__ float fold16(float a, float b) {
  auto s = __builtin_amdgcn_permlane16_swap(__float_as_uint(a), __float_as_uint(b), false, false);
  return __uint_as_float(s[0]) + __uint_as_float(s[1]);
}
__device__ __forceinline__ float xmax_rows(float x) {
  auto s = __builtin_amdgcn_permlane16_swap(__float_as_uint(x), __float_as_uint(x), false, false);
  x = fmaxf(__uint_as_float(s[0]), __uint_as_float(s[1]));
  auto t = __builtin_amdgcn_permlane32_swap(__float_as_uint(x), __float_as_uint(x), false, false);
  return fmaxf(__uint_as_float(t[0]), __uint_as_float(t[1]));
}
__device__ __forceinline__ unsigned cvt2(float lo, float hi) {
  typedef __bf16 bf16x2 __attribute__((ext_vector_type(2)));
  bf16x2 v = {(__bf16)lo, (__bf16)hi};
  return __builtin_bit_cast(unsigned, v);
}
__device__ __forceinline__ float rbf(float x) { return __uint_as_float(cvt2(x, x) << 16); }
__device__ __forceinline__ float bflo(unsigned v) { return __uint_as_float(v << 16); }
__device__ __forceinline__ float bfhi(unsigned v) { return __uint_as_float(v & 0xffff0000u); }
__device__ __forceinline__ u32x4 trfrag(unsigned a0, unsigned a1) {
  u32x2 lo = __builtin_bit_cast(u32x2, __builtin_amdgcn_ds_read_tr16_b64_v4i16((LAS s16x4*)(uintptr_t)a0));
  u32x2 hi = __builtin_bit_cast(u32x2, __builtin_amdgcn_ds_read_tr16_b64_v4i16((LAS s16x4*)(uintptr_t)a1));
  return u32x4{lo.x, lo.y, hi.x, hi.y};
}
__device__ __forceinline__ f32x4 mfma(u32x4 a, u32x4 b, f32x4 c) {
  return __builtin_amdgcn_mfma_f32_16x16x32_bf16(__builtin_bit_cast(bf16x8, a), __builtin_bit_cast(bf16x8, b), c, 0, 0, 0);
}
template <typename T> __device__ __forceinline__ T lds_ld(unsigned a) { return *(const LAS T*)(uintptr_t)a; }
template <typename T> __device__ __forceinline__ void lds_st(unsigned a, T v) { *(LAS T*)(uintptr_t)a = v; }
__device__ __forceinline__ unsigned nosent(float x) { return min(__float_as_uint(x), 0xfffffffeu); }
__device__ __forceinline__ unsigned umax4(u32x4 v) { return max(max(v.x, v.y), max(v.z, v.w)); }
__device__ __forceinline__ void flag_set(unsigned addr, unsigned val) {
  asm volatile("ds_write_b32 %0, %1" ::"v"(addr), "v"(val) : "memory");
}
__device__ __forceinline__ u32x4 flag_get4(unsigned addr) {
  u32x4 r;
  asm volatile("ds_read_b128 %0, %1\n\ts_waitcnt lgkmcnt(0)" : "=&v"(r) : "v"(addr) : "memory");
  return r;
}
template <int MASK> __device__ __forceinline__ void flag_wait(unsigned addr, unsigned lvl) {
#pragma unroll 1
  for (unsigned spin = 0; spin < (SPIN_CAP << 4); ++spin) {
    const u32x4 f = flag_get4(addr);
    unsigned m = ~0u;
    if (MASK & 1) m = min(m, f.x);
    if (MASK & 2) m = min(m, f.y);
    if (MASK & 4) m = min(m, f.z);
    if (MASK & 8) m = min(m, f.w);
    if ((unsigned)__builtin_amdgcn_readfirstlane(m) >= lvl) break;
  }
}
__device__ __forceinline__ void loaders_wait(unsigned laddr, unsigned lvl) {
#pragma unroll 1
  for (unsigned spin = 0; spin < (SPIN_CAP << 4); ++spin) {
    unsigned f;
    asm volatile("ds_read_b32 %0, %1\n\ts_waitcnt lgkmcnt(0)" : "=&v"(f) : "v"(laddr) : "memory");
    if (!__any(f < lvl)) break;
  }
}

__device__ __forceinline__ unsigned f24(float v) { return (__float_as_uint(v) + 0x80u) >> 8; }
__device__ __forceinline__ f32x4 sel4(f32x4 c0, f32x4 c1, f32x4 c2, f32x4 c3, unsigned p4) {
  f32x4 c01 = (p4 & 1) ? c1 : c0, c23 = (p4 & 1) ? c3 : c2;
  asm volatile("" : "+v"(c01), "+v"(c23));
  return (p4 & 2) ? c23 : c01;
}
__device__ __forceinline__ unsigned gbad(u32x4 v, unsigned tl, unsigned th) { return (((v.x ^ tl) | (v.z ^ tl)) >> 24) | (v.y ^ th) | (v.w ^ th); }
__device__ __forceinline__ float gval(unsigned v) { return __uint_as_float(v << 8); }

extern "C" __global__ void __launch_bounds__(832, 4)
gated_mix_kernel(const u16* a_x, const u16* a_kc, const u16* a_vc, const u16* a_wq, const u16* a_wo, const u16* a_w1, const u16* a_b1,
                 const u16* a_w2, const u16* a_b2, const u16* a_gw, u16* a_out, unsigned char* a_ws) {
  __shared__ __attribute__((aligned(16))) unsigned char lds[LDS_BYTES];
  asm volatile("" ::"v"((LAS unsigned char*)lds) : "memory");
  const unsigned tid = threadIdx.x, lane = tid & 63, w = __builtin_amdgcn_readfirstlane(tid >> 6);
  ST(0);
  const u64 tstart_ = __builtin_amdgcn_s_memrealtime();
  u32x16 ka;
  u32x8 kb;
  asm volatile("s_load_dwordx16 %0, %2, 0x0\n\ts_load_dwordx8 %1, %2, 0x40" : "=&s"(ka), "=&s"(kb) : "s"(__builtin_amdgcn_kernarg_segment_ptr()));
  const unsigned bid = blockIdx.x, g = bid & 7, j = bid >> 3;
  const unsigned hi = lane >> 5, s32 = lane & 31, grp = lane >> 4, r16 = lane & 15, q4 = r16 >> 2, p4 = lane & 3;
  const size_t kvoff = ((size_t)g * SK + (SK - WIN) + j * 128) * 512;
  const unsigned jt = min(j, 27u), rb = jt / 7, cb = jt % 7;
  const unsigned hb = min(4 * g + rb, 27u);
  const unsigned fin = min(4 * g + j, 27u);
  const unsigned t1 = g * 28 + jt;
  const unsigned zero = 0;
  const f32x4 z4 = {0.f, 0.f, 0.f, 0.f};
  const u32x4 zu = {0u, 0u, 0u, 0u};
  const unsigned c = w - 1, u = w - 5, lfa = F_L + (lane & 7) * 4;

  const unsigned qoff = ((j & 3) * 112 + u * 16) * 8192 + g * 1024 + (j >> 2) * 128;
  const unsigned hoff = ((t1 & 7) * 56 + u * 8) * 3584 + (t1 >> 3) * 128;
  const unsigned tv = (lane >> 3) * 8192 + (lane & 7) * 16, tw = (lane >> 3) * 3584 + (lane & 7) * 16;
  const size_t koff = kvoff + (u << 10);
  unsigned dl = K_OFF + u * 1024;
  unsigned dv = hi * 512 + ((s32 ^ hi ^ (u << 1)) << 4);
  asm volatile("s_waitcnt lgkmcnt(0)" : "+s"(ka), "+s"(kb) : "s"(qoff), "s"(hoff), "v"(tv), "v"(tw), "s"(koff), "s"(dl), "v"(dv));
#define KPTR(v, i) ((unsigned char*)(((u64)v[2 * (i) + 1] << 32) | v[2 * (i)]))
  const unsigned char *x = KPTR(ka, 0), *kc = KPTR(ka, 1), *vc = KPTR(ka, 2), *wq = KPTR(ka, 3), *wo = KPTR(ka, 4), *w1 = KPTR(ka, 5), *b1 = KPTR(ka, 6),
                      *w2 = KPTR(ka, 7), *b2 = KPTR(kb, 0), *gw = KPTR(kb, 1), *out = KPTR(kb, 2);
  unsigned char* ws = KPTR(kb, 3);
#define FINISH()
#define STI(i)
  ST(16);
  const unsigned char* dsrc = kc + koff;
#define STEP() do { DMA1(dv, dsrc, dl); dsrc += 8192; dl += 8192; } while (0)
#define LOOP(cnt, body) _Pragma("unroll 1") for (unsigned i_ = (cnt); i_ != 0; --i_) { body; }
  u32x2 ep;
  u32x4 xf0, xf1, xf2, xf3;
  unsigned b2v, b1v;
  if (w >= 5) {
    if (u < 7) {
      asm volatile("s_mov_b32 m0, %3\n\ts_nop 0\n\tglobal_load_lds_dwordx4 %0, %2 nt\n\tglobal_load_lds_dwordx4 %1, %2 offset:1024 nt" ::"v"(tv), "v"(tv + 65536 - 1024),
                   "s"(wq + qoff), "s"(WQ_OFF + u * 2048) : "memory");
      DMA1(tw, w1 + hoff, W1_OFF + u * 1024);
    }
    LOOP(WK, STEP());
    asm volatile("" : "=v"(ep), "=v"(xf0), "=v"(xf1), "=v"(xf2), "=v"(xf3), "=v"(b2v), "=v"(b1v));
  } else {
    asm volatile("" : "=v"(ep), "=v"(xf0), "=v"(xf1), "=v"(xf2), "=v"(xf3), "=v"(b2v), "=v"(b1v));
    const unsigned vx = w == 3 ? min(lane, 55u) * 16 : grp * 16 + (w == 0 ? (j & 3) * 224 : (t1 & 7) * 112);
    if (w == 0 || w == 2) LDX2(ep, zero, ws + WS_EPOCH, "sc1");
    if (w != 1 && w != 4) {
      LDX4(xf0, vx, x, "");
      LDX4(xf1, vx + (w == 0 ? 64 : w == 3 ? 0 : 48), w == 3 ? gw : x, "");
    }
    if (w == 4) {
      const u32x4 sent = {~0u, ~0u, ~0u, ~0u};
      ST4(grp * 8192 + r16 * 16, sent, ws + WS_IN1 + g * 32768 + j * 256, "sc1");
    }
    if (w == 0) {
      LDX4(xf2, vx, x, "offset:128");
      LDX4(xf3, vx, x, "offset:160");
      if (lane < 20) lds_st<unsigned>(lane * 4, 0u);
      if (lane < 8) lds_st<unsigned>(MX + lane * 4, ~0u);
      lds_st<u32x2>(PS + lane * 8, u32x2{~0u, ~0u});
      lds_st<u32x4>(RED + lane * 16, u32x4{~0u, ~0u, ~0u, ~0u});
      asm volatile("s_waitcnt lgkmcnt(0)" ::: "memory");
    }
  }
  ST(17);
  asm volatile("s_barrier" ::: "memory");
  if (__builtin_expect_with_probability(w >= 5, 1, 0.99)) {
    const unsigned fa = F_L + u * 4;
    WAITV(WK);
    flag_set(fa, 1u);
    LOOP(8 - WK, STEP(); WAITV(WK));
    ST(1);
    dv = hi * 512 + ((s32 ^ ((hi | ((u & 3) << 1)) << 1)) << 4);
    dsrc = vc + koff; dl = V_OFF + u * 1024;
    LOOP(WV, STEP(); WAITV(WV));
    flag_set(fa, 2u);
    LOOP(8 - WV, STEP(); WAITV(WV2));
    if (j >= 28) {
      WAITV(2);
      flag_set(fa, 3u);
      WAITV(0);
      flag_set(fa, 4u);
    } else {
      flag_wait<3>(F_R, 1u);
      const unsigned char* po = wo + (size_t)(g * 512 + rb * 128 + u * 8) * 896 + cb * 128;
      const unsigned wv = (lane >> 3) * 896 + (lane & 7) * 16;
      DMA1(wv, po, WO_OFF + u * 1024);
      DMA1(wv, po + 64 * 896, WO_OFF + 8192 + u * 1024);
      DMA1(wv, w2 + (size_t)(hb * 64 + u * 8) * 896 + cb * 128, W2_OFF + u * 1024);
      WAITV(5);
      flag_set(fa, 3u);
      WAITV(3);
      flag_set(fa, 4u);
      ST(2);
      WAITV(0);
      flag_set(fa, 5u);
      ST(3);
    }
    FINISH();
    return;
  }
  ST(13);
  asm volatile("s_waitcnt vmcnt(0)" : "+v"(ep), "+v"(xf0), "+v"(xf1), "+v"(xf2), "+v"(xf3)::"memory");
  const u64 e1 = (((u64)(unsigned)__builtin_amdgcn_readfirstlane(ep.y) << 32) | (unsigned)__builtin_amdgcn_readfirstlane(ep.x)) + 1ull;
  const unsigned e_lo = (unsigned)e1, e_hi = (unsigned)(e1 >> 32);
  const unsigned tl = e_lo << 24, th = (unsigned)(e1 >> 8) << 1, tq = e_lo << 1;

  if (w == 0) {
    xf3 = hi ? xf3 : zu;
    loaders_wait(lfa, 1u);
    ST(2);
    {
      const unsigned a = WQ_OFF + (8 * grp + q4) * 128 + p4 * 32;
      f32x4 acc[4];
#pragma unroll
      for (int mu = 0; mu < 4; ++mu) {
        acc[mu] = mfma(trfrag(a + mu * 8, a + mu * 8 + 512), xf0, z4);
        acc[mu] = mfma(trfrag(a + 4096 + mu * 8, a + 4096 + mu * 8 + 512), xf1, acc[mu]);
        acc[mu] = mfma(trfrag(a + 8192 + mu * 8, a + 8192 + mu * 8 + 512), xf2, acc[mu]);
        acc[mu] = mfma(trfrag(a + 10240 + mu * 8, a + 10240 + mu * 8 + 512), xf3, acc[mu]);
      }
      const f32x4 v = sel4(acc[0], acc[1], acc[2], acc[3], p4);
      const u32x4 gr = {cvt2(v.x, v.y), tq, cvt2(v.z, v.w), tq};
      if (r16 < 4) ST4(grp * 64 + p4 * 16, gr, ws + WS_QG + g * 8192 + (j & 3) * 2048 + (j >> 2) * 256, "sc1");
      flag_set(F_R, 1u);
    }
    ST(3);
    {
      const unsigned char* qgp = uptr(ws + WS_QG + g * 8192);
      u32x4 qa[8];
      __builtin_amdgcn_s_sleep(QD0);
      ST(15);
#pragma unroll 1
      for (unsigned spin = 0; spin < SPIN_CAP; ++spin) {
#pragma unroll
        for (int i = 0; i < 2; ++i) {
          const unsigned vo_ = lane * 16 + i * 4096;
          LD4(qa[4 * i], vo_, qgp, "sc1"); LD4(qa[4 * i + 1], vo_, qgp, "offset:1024 sc1"); LD4(qa[4 * i + 2], vo_, qgp, "offset:2048 sc1"); LD4(qa[4 * i + 3], vo_, qgp, "offset:3072 sc1");
        }
        asm volatile("s_waitcnt vmcnt(0)" : "+v"(qa[0]), "+v"(qa[1]), "+v"(qa[2]), "+v"(qa[3]), "+v"(qa[4]), "+v"(qa[5]), "+v"(qa[6]), "+v"(qa[7])::"memory");
        unsigned bad = 0;
#pragma unroll
        for (int i = 0; i < 8; ++i) bad |= (qa[i].y ^ tq) | (qa[i].w ^ tq);
        if (!__any(bad != 0u)) break;
      }
#pragma unroll
      for (int h = 0; h < 2; ++h) {
        const float v0 = (bflo(qa[h].x) + bflo(qa[2 + h].x)) + (bflo(qa[4 + h].x) + bflo(qa[6 + h].x));
        const float v1 = (bfhi(qa[h].x) + bfhi(qa[2 + h].x)) + (bfhi(qa[4 + h].x) + bfhi(qa[6 + h].x));
        const float v2 = (bflo(qa[h].z) + bflo(qa[2 + h].z)) + (bflo(qa[4 + h].z) + bflo(qa[6 + h].z));
        const float v3 = (bfhi(qa[h].z) + bfhi(qa[2 + h].z)) + (bfhi(qa[4 + h].z) + bfhi(qa[6 + h].z));
        lds_st<u32x2>(QS + h * 576 + lane * 8, u32x2{cvt2(v0, v1), cvt2(v2, v3)});
      }
      flag_set(F_Q, 1u);
    }
    ST(4);
    LDU(b2v, ((lane & 7) * 2 + hi) * 2, b2 + fin * 32, "");
    asm volatile("global_load_dword %0, %2, %3\n\ts_waitcnt vmcnt(0)" : "=&v"(b1v), "+v"(b2v) : "v"(s32 * 4), "s"(b1 + hb * 128) : "memory");
    {
      flag_wait<15>(F_C, 2u);
      const f32x4 mm = lds_ld<f32x4>(MX + (lane & 1) * 16), ls = lds_ld<f32x4>(SUMS + (lane & 1) * 16);
      const u32x4 ml = {tl | f24(fmaxf(fmaxf(mm.x, mm.y), fmaxf(mm.z, mm.w))), th, tl | f24((ls.x + ls.y) + (ls.z + ls.w)), th};
      if (lane < 2) ST4(lane * 512, ml, ws + WS_ML + g * 1024 + j * 16, "sc1");
    }
    ST(6);
    if (j < 28) {
      const f32x2 gt = lds_ld<f32x2>(GATE);
      {
        const unsigned char* hp = ws + WS_HG + hb * 4096;
        u32x4 h0, h1, h2, h3;
#pragma unroll 1
        for (unsigned spin = 0; spin < SPIN_CAP; ++spin) {
          LD4(h0, lane * 16, hp, "sc1"); LD4(h1, lane * 16, hp, "offset:1024 sc1"); LD4(h2, lane * 16, hp, "offset:2048 sc1"); LD4(h3, lane * 16, hp, "offset:3072 sc1");
          asm volatile("s_waitcnt vmcnt(0)" : "+v"(h0), "+v"(h1), "+v"(h2), "+v"(h3)::"memory");
          if (!__any((gbad(h0, tl, th) | gbad(h1, tl, th) | gbad(h2, tl, th) | gbad(h3, tl, th)) != 0u)) break;
        }
        float a0 = (gval(h0.x) + gval(h1.x)) + (gval(h2.x) + gval(h3.x)), a1 = (gval(h0.z) + gval(h1.z)) + (gval(h2.z) + gval(h3.z));
        a0 = fold32(a0, a0); a1 = fold32(a1, a1);
        a0 = rbf(rbf(a0) + bflo(b1v)); a1 = rbf(rbf(a1) + bfhi(b1v));
        a0 = a0 > 0.f ? a0 : 0.f; a1 = a1 > 0.f ? a1 : 0.f;
        const float sc = g < 7 ? gt.y : 0.f;
        if (lane < 32) lds_st<unsigned>(HS + lane * 4, cvt2(sc * rbf(a0 * a0), sc * rbf(a1 * a1)));
        flag_set(F_Q + 8, 1u);
      }
      {
        const unsigned char* mlp = ws + WS_ML + (g * 2 + (rb >> 1)) * 512;
        u32x4 ml;
#pragma unroll 1
        for (unsigned spin = 0; spin < SPIN_CAP; ++spin) {
          LD4(ml, s32 * 16, mlp, "sc1");
          asm volatile("s_waitcnt vmcnt(0)" : "+v"(ml)::"memory");
          if (!__any(gbad(ml, tl, th) != 0u)) break;
          __builtin_amdgcn_s_sleep(4);
        }
        const float m0 = gval(ml.x);
        const float Mx = xmax_rows(row_max(m0));
        const float w0 = __builtin_amdgcn_exp2f(m0 - Mx);
        float L = row_sum(w0 * gval(ml.z));
        L = fold16(L, L);
        const float ww = w0 * (gt.x / L);
        if (lane < 32) lds_st<u16>(WSC + lane * 2, (u16)cvt2(ww, ww));
        flag_set(F_Q + 12, 1u);
      }
      ST(9);
      const unsigned char* o2 = ws + WS_IN2 + cb * 16384 + (g * 4 + rb) * 512;
      const unsigned char* in2 = ws + WS_IN2 + (fin >> 2) * 16384 + (fin & 3) * 128;
      const unsigned ra2 = RED + s32 * 8, vo2 = (lane >> 3) * 512 + (lane & 7) * 16;
      const float ob = gt.y * bflo(b2v);
      asm volatile("" ::"s"(o2), "s"(in2), "v"(ra2), "v"(vo2), "v"(ob));
      u32x2 r0, r1, r2, r3;
#pragma unroll 1
      for (unsigned spin = 0; spin < (SPIN_CAP << 4); ++spin) {
        asm volatile("ds_read_b64 %0, %4\n\tds_read_b64 %1, %4 offset:256\n\tds_read_b64 %2, %4 offset:512\n\tds_read_b64 %3, %4 offset:768\n\ts_waitcnt lgkmcnt(0)"
                     : "=&v"(r0), "=&v"(r1), "=&v"(r2), "=&v"(r3) : "v"(ra2) : "memory");
        if (!__any(max(max(r0.x, r1.x), max(r2.x, r3.x)) == ~0u)) break;
      }
      const f32x2 r = (__builtin_bit_cast(f32x2, r0) + __builtin_bit_cast(f32x2, r1)) + (__builtin_bit_cast(f32x2, r2) + __builtin_bit_cast(f32x2, r3));
      const u32x4 o = {tl | f24(r.x), th, tl | f24(r.y), th};
      if (lane < 32) ST4(lane * 16, o, o2, "sc1");
      ST(11);
      if (j < 4 && g < 7) {
        u32x4 v0, v1, v2, v3;
        __builtin_amdgcn_s_sleep(HD2);
#pragma unroll 1
        for (unsigned spin = 0; spin < SPIN_CAP; ++spin) {
          LD4(v0, vo2, in2, "sc1"); LD4(v1, vo2 + 4096, in2, "sc1"); LD4(v2, vo2 + 8192, in2, "sc1"); LD4(v3, vo2 + 12288, in2, "sc1");
          asm volatile("s_waitcnt vmcnt(0)" : "+v"(v0), "+v"(v1), "+v"(v2), "+v"(v3)::"memory");
          if (!__any((gbad(v0, tl, th) | gbad(v1, tl, th) | gbad(v2, tl, th) | gbad(v3, tl, th)) != 0u)) break;
        }
        ST(12);
        const float a0 = (gval(v0.x) + gval(v1.x)) + (gval(v2.x) + gval(v3.x)), a1 = (gval(v0.z) + gval(v1.z)) + (gval(v2.z) + gval(v3.z));
        float tt = fold32(a0, a1);
        tt = fold16(tt, tt);
        tt += dpp<ROR8>(tt);
        tt += ob;
        const unsigned ob2 = cvt2(tt, tt);
        { unsigned n_ = 0; while (__builtin_amdgcn_s_memrealtime() - tstart_ < 1100ull && ++n_ < 100000u) __builtin_amdgcn_s_sleep(1); }
        if ((lane & 24) == 0) STH(((lane & 7) * 2 + hi) * 2, ob2, out + fin * 32, "sc1");
        if (bid == 0 && lane == 0) { const u32x2 e = {e_lo, e_hi}; ST2(zero, e, ws + WS_EPOCH, "sc1"); }
      }
    }
  } else {
    if (w == 2) {
      xf1 = grp ? xf1 : zu;
      loaders_wait(lfa, 1u);
      const unsigned a = W1_OFF + (8 * grp + q4) * 128 + p4 * 8;
      f32x4 acc[4];
#pragma unroll
      for (int t = 0; t < 4; ++t) {
        acc[t] = mfma(xf0, trfrag(a + t * 32, a + t * 32 + 512), z4);
        acc[t] = mfma(xf1, trfrag(a + t * 32 + 3072, a + t * 32 + 3584), acc[t]);
      }
      const float v = hi ? ((grp & 1) ? acc[3].x : acc[2].x) : ((grp & 1) ? acc[1].x : acc[0].x);
      const u32x2 gr = {tl | f24(v), th};
      if (j < 28) ST2(lane * 8, gr, ws + WS_HG + t1 * 512, "sc1");
      flag_set(F_R + 4, 1u);
    } else if (w == 3) {
      float z = 0.f;
#pragma unroll
      for (int i = 0; i < 4; ++i) {
        const unsigned pr = cvt2(bflo(xf0[i]) * bflo(xf1[i]), bfhi(xf0[i]) * bfhi(xf1[i]));
        z += bflo(pr); z += bfhi(pr);
      }
      z = lane < 56 ? z : 0.f;
      z = row_sum(z);
      z = fold16(z, z); z = fold32(z, z);
      z = rbf(z);
      const float gate = rbf(1.f / (1.f + __expf(-z)));
      if (lane == 0) lds_st<f32x2>(GATE, f32x2{gate, rbf(1.f - gate)});
    }
    loaders_wait(lfa, 2u);
    ST(2);
    u32x4 kf[16];
    {
      const unsigned a = K_OFF + c * 16384 + r16 * 512 + ((grp ^ r16) << 4);
#pragma unroll
      for (int st = 0; st < 16; ++st) kf[st] = lds_ld<u32x4>((a ^ ((st & 3) << 6)) + ((st >> 2) & 1) * 256 + (st >> 3) * 8192);
    }
    const unsigned qaddr = QS + ((r16 >> 2) & 1) * 576 + grp * 16, hsel = ((lane >> 2) & 1) * 16, pa = PS + ((r16 >> 2) & 1) * 64 + grp * 16;
    const unsigned sg = (q4 | ((grp & 1) << 2)) << 1;
    const unsigned a0 = V_OFF + (4 * grp + q4) * 512 + (((8 * c + 2 * p4) ^ sg) << 4);
    const unsigned off = (r16 >> 2) * 16384 + grp * 32 + p4 * 8;
    const unsigned char* o1 = ws + WS_IN1 + (g * 4 + (c >> 1)) * 8192 + j * 256 + (c & 1) * 128;
    asm volatile("" ::"v"(qaddr), "s"(o1));
    STI(4);
    flag_wait<1>(F_Q, 1u);
    ST(5);
    f32x4 sa = z4, sb = z4;
    {
      u32x4 qf[8];
#pragma unroll
      for (int st = 0; st < 8; ++st) qf[st] = lds_ld<u32x4>(qaddr + st * 64);
      asm volatile("" : "+v"(qf[0]), "+v"(qf[1]), "+v"(qf[2]), "+v"(qf[3]), "+v"(qf[4]), "+v"(qf[5]), "+v"(qf[6]), "+v"(qf[7]));
#pragma unroll
      for (int st = 0; st < 8; ++st) {
        sa = mfma(kf[st], qf[st], sa);
        sb = mfma(kf[8 + st], qf[st], sb);
      }
    }
    STI(20);
    constexpr float C2 = 0.0625f * 1.4426950408889634f;
    const float m = xmax_rows(fmaxf(fmaxf(fmaxf(sa.x, sa.y), fmaxf(sa.z, sa.w)), fmaxf(fmaxf(sb.x, sb.y), fmaxf(sb.z, sb.w)))) * C2;
    if ((lane & 59) == 0) lds_st<unsigned>(MX + hsel + c * 4, f24(m) << 8);
    STI(21);
    u32x4 mm;
#pragma unroll 1
    for (unsigned spin = 0; spin < (SPIN_CAP << 4); ++spin) {
      mm = flag_get4(MX + hsel);
      if (!__any(umax4(mm) == ~0u)) break;
    }
    STI(22);
    const float M = fmaxf(fmaxf(__uint_as_float(mm.x), __uint_as_float(mm.y)), fmaxf(__uint_as_float(mm.z), __uint_as_float(mm.w)));
    float t[8];
#pragma unroll
    for (int i = 0; i < 4; ++i) { t[i] = __builtin_amdgcn_exp2f(fmaf(sa[i], C2, -M)); t[4 + i] = __builtin_amdgcn_exp2f(fmaf(sb[i], C2, -M)); }
    if ((r16 & 11) == 0) lds_st<u32x4>(pa + c * 128, u32x4{min(cvt2(t[0], t[1]), 0xfffffffeu), cvt2(t[2], t[3]), cvt2(t[4], t[5]), cvt2(t[6], t[7])});
    float l = ((t[0] + t[1]) + (t[2] + t[3])) + ((t[4] + t[5]) + (t[6] + t[7]));
    l = fold16(l, l); l = fold32(l, l);
    if ((lane & 59) == 0) lds_st<float>(SUMS + hsel + c * 4, l);
    flag_set(F_C + c * 4, 2u);
    STI(23);
    f32x4 cc[4] = {z4, z4, z4, z4};
    u32x4 pf[4];
#pragma unroll 1
    for (unsigned spin = 0; spin < (SPIN_CAP << 4); ++spin) {
      asm volatile("ds_read_b128 %0, %4\n\tds_read_b128 %1, %4 offset:128\n\tds_read_b128 %2, %4 offset:256\n\tds_read_b128 %3, %4 offset:384\n\ts_waitcnt lgkmcnt(0)"
                   : "=&v"(pf[0]), "=&v"(pf[1]), "=&v"(pf[2]), "=&v"(pf[3]) : "v"(pa) : "memory");
      if (!__any(max(max(pf[0].x, pf[1].x), max(pf[2].x, pf[3].x)) == ~0u)) break;
    }
    ST(6);
    loaders_wait(lfa, 3u);
    STI(18);
#pragma unroll
    for (int ks = 0; ks < 3; ++ks)
#pragma unroll
      for (int mu = 0; mu < 4; ++mu) cc[mu] = mfma(trfrag(a0 + ks * 16384 + mu * 8, a0 + ks * 16384 + 8192 + mu * 8), pf[ks], cc[mu]);
    asm volatile("" : "+v"(cc[0]), "+v"(cc[1]), "+v"(cc[2]), "+v"(cc[3]), "+v"(pf[3]) : "v"(off));
    STI(7);
    loaders_wait(lfa, 4u);
    STI(19);
    {
      u32x4 vf[4];
#pragma unroll
      for (int mu = 0; mu < 4; ++mu) vf[mu] = trfrag(a0 + 49152 + mu * 8, a0 + 57344 + mu * 8);
      asm volatile("" : "+v"(vf[0]), "+v"(vf[1]), "+v"(vf[2]), "+v"(vf[3]));
#pragma unroll
      for (int mu = 0; mu < 4; ++mu) cc[mu] = mfma(vf[mu], pf[3], cc[mu]);
      const f32x4 cs = sel4(cc[0], cc[1], cc[2], cc[3], p4);
      const u32x2 o = {min(cvt2(cs.x, cs.y), 0xfffffffeu), min(cvt2(cs.z, cs.w), 0xfffffffeu)};
      if (r16 < 8) ST2(off, o, o1, "sc1");
    }
    ST(8);
    if (j < 28) {
      loaders_wait(lfa, 5u);
      f32x4 acc[4];
      u32x4 wf[4];
      const unsigned ra = WO_OFF + (32 * c + 4 * grp + q4) * 128 + p4 * 8;
#pragma unroll
      for (int t2 = 0; t2 < 4; ++t2) { wf[t2] = trfrag(ra + t2 * 32, ra + t2 * 32 + 2048); acc[t2] = z4; }
      if (c == 1 || c == 2) {
        flag_wait<4>(F_Q, 1u);
        const unsigned rh = W2_OFF + (32 * (c - 1) + 8 * grp + q4) * 128 + p4 * 8;
        const u32x4 hf = lds_ld<u32x4>(HS + (c - 1) * 64 + grp * 16);
#pragma unroll
        for (int t2 = 0; t2 < 4; ++t2) acc[t2] = mfma(trfrag(rh + t2 * 32, rh + t2 * 32 + 512), hf, acc[t2]);
      }
      flag_wait<8>(F_Q, 1u);
      const u32x4 wsf = lds_ld<u32x4>(WSC + grp * 16);
      const unsigned char* in1 = ws + WS_IN1 + (g * 4 + rb) * 8192 + c * 64;
      const unsigned vo = (lane >> 2) * 256 + p4 * 16;
      const unsigned sw = STG + c * 2048 + (lane >> 2) * 64 + ((p4 ^ (hi << 1)) << 4);
      const unsigned sr = STG + c * 2048 + (8 * grp + q4) * 64 + (grp & 1) * 32 + p4 * 8;
      const unsigned rw = RED + c * 256 + grp * 16;
      asm volatile("" : "+v"(wf[0]), "+v"(wf[1]), "+v"(wf[2]), "+v"(wf[3]), "+v"(acc[0]), "+v"(acc[1]), "+v"(acc[2]), "+v"(acc[3]) : "v"(wsf), "v"(vo), "v"(sw), "v"(sr), "v"(rw), "s"(in1));
      ST(9);
      __builtin_amdgcn_s_sleep(HD1);
      u32x4 v0, v1;
#pragma unroll 1
      for (unsigned spin = 0; spin < SPIN_CAP; ++spin) {
        LD4(v0, vo, in1, "sc1");
        LD4(v1, vo + 4096, in1, "sc1");
        asm volatile("s_waitcnt vmcnt(0)" : "+v"(v0), "+v"(v1)::"memory");
        if (!__any(max(umax4(v0), umax4(v1)) == ~0u)) break;
      }
      ST(10);
      lds_st<u32x4>(sw, v0);
      lds_st<u32x4>(sw + 1024, v1);
      const f32x4 oa = mfma(trfrag(sr, sr + 256), wsf, z4), ob = mfma(trfrag(sr ^ 32, (sr ^ 32) + 256), wsf, z4);
      const u32x4 of = {cvt2(oa.x, oa.y), cvt2(oa.z, oa.w), cvt2(ob.x, ob.y), cvt2(ob.z, ob.w)};
#pragma unroll
      for (int t2 = 0; t2 < 4; ++t2) acc[t2] = mfma(wf[t2], of, acc[t2]);
      if (r16 == 0) {
#pragma unroll
        for (int t2 = 0; t2 < 4; ++t2) lds_st<u32x4>(rw + t2 * 64, u32x4{nosent(acc[t2].x), __float_as_uint(acc[t2].y), nosent(acc[t2].z), __float_as_uint(acc[t2].w)});
      }
      ST(11);
    }
  }
  FINISH();
}

extern "C" void kernel_launch(void* const* d_in, const int* in_sizes, int n_in, void* d_out, int out_size, void* d_ws,
                              size_t ws_size, hipStream_t stream) {
  gated_mix_kernel<<<256, 832, 0, stream>>>((const u16*)d_in[0], (const u16*)d_in[1], (const u16*)d_in[2], (const u16*)d_in[3],
                                            (const u16*)d_in[4], (const u16*)d_in[6], (const u16*)d_in[7], (const u16*)d_in[8],
                                            (const u16*)d_in[9], (const u16*)d_in[10], (u16*)d_out, (unsigned char*)d_ws);
}
```

```cpp
#include <hip/hip_runtime.h>
#include <hip/hip_bf16.h>
#include <stdint.h>

typedef unsigned long long u64;
typedef unsigned short u16;
typedef short s16x4 __attribute__((ext_vector_type(4)));
typedef __bf16 bf16x8 __attribute__((ext_vector_type(8)));
typedef float f32x4 __attribute__((ext_vector_type(4)));
typedef float f32x2 __attribute__((ext_vector_type(2)));
typedef unsigned u32x4 __attribute__((ext_vector_type(4)));
typedef unsigned u32x2 __attribute__((ext_vector_type(2)));
typedef unsigned u32x8 __attribute__((ext_vector_type(8)));
typedef unsigned u32x16 __attribute__((ext_vector_type(16)));
#define LAS __attribute__((address_space(3)))

constexpr int SK = 65536, WIN = 4096;
constexpr unsigned F_L = 0  , F_C = 32  , F_Q = 48  , F_R = 64  ;
constexpr unsigned C_T = 80  , C_K = 84, C_V1 = 88, C_V2 = 92, C_W = 96, C_X = 100  ;
constexpr unsigned GATE = 112  , MX = 128, SUMS = 160, WSC = 192  , QS = 512  , PS = 1792  ,
                   HS = 2432  , RED = 2560  ;
constexpr unsigned WQ_OFF = 4096, W1_OFF = 18432, WO_OFF = 4096, W2_OFF = 20480, K_OFF = 32768, V_OFF = 98304, STG = K_OFF;
constexpr int LDS_BYTES = 163840;
constexpr unsigned WS_EPOCH = 0, WS_ML = 8192  , WS_HG = 131072  , WS_IN1 = 262144  , WS_IN2 = 524288  ,
                   WS_QG = 655360  ;
constexpr unsigned SPIN_CAP = 1u << 19;

#define WAITV(n) asm volatile("s_waitcnt vmcnt(%0)" ::"i"(n) : "memory")

__device__ __forceinline__ const unsigned char* uptr(const void* p) {
  const u64 a = (u64)p;
  return (const unsigned char*)(((u64)(unsigned)__builtin_amdgcn_readfirstlane((unsigned)(a >> 32)) << 32) | (unsigned)__builtin_amdgcn_readfirstlane((unsigned)a));
}
#define DMA1(v0, sb, lds) \
  asm volatile("s_mov_b32 m0, %2\n\ts_nop 0\n\tglobal_load_lds_dwordx4 %0, %1 nt" ::"v"(v0), "s"(sb), "s"(lds) : "memory")
#define LD4(dst, voff, sb, mods) asm volatile("global_load_dwordx4 %0, %1, %2 " mods : "=v"(dst) : "v"(voff), "s"(sb) : "memory")
#define LDX4(dst, voff, sb, mods) asm volatile("global_load_dwordx4 %0, %1, %2 " mods : "+v"(dst) : "v"(voff), "s"(sb) : "memory")
#define LDX2(dst, voff, sb, mods) asm volatile("global_load_dwordx2 %0, %1, %2 " mods : "+v"(dst) : "v"(voff), "s"(sb) : "memory")
#define LDU(dst, voff, sb, mods) asm volatile("global_load_ushort %0, %1, %2 " mods : "=v"(dst) : "v"(voff), "s"(sb) : "memory")
#define ST4(voff, val, sb, mods) asm volatile("global_store_dwordx4 %0, %1, %2 " mods "\n\ts_nop 1" ::"v"(voff), "v"(val), "s"(sb) : "memory")
#define ST2(voff, val, sb, mods) asm volatile("global_store_dwordx2 %0, %1, %2 " mods ::"v"(voff), "v"(val), "s"(sb) : "memory")
#define STH(voff, val, sb, mods) asm volatile("global_store_short %0, %1, %2 " mods ::"v"(voff), "v"(val), "s"(sb) : "memory")

template <int CTRL> __device__ __forceinline__ float dpp(float x) {
  return __builtin_bit_cast(float, __builtin_amdgcn_mov_dpp(__builtin_bit_cast(int, x), CTRL, 0xf, 0xf, true));
}
constexpr int ROR8 = 0x128, ROR4 = 0x124, ROR2 = 0x122, ROR1 = 0x121;
__device__ __forceinline__ float row_max(float x) { x = fmaxf(x, dpp<ROR8>(x)); x = fmaxf(x, dpp<ROR4>(x)); x = fmaxf(x, dpp<ROR2>(x)); return fmaxf(x, dpp<ROR1>(x)); }
__device__ __forceinline__ float row_sum(float x) { x += dpp<ROR8>(x); x += dpp<ROR4>(x); x += dpp<ROR2>(x); return x + dpp<ROR1>(x); }
__device__ __forceinline__ float fold32(float a, float b) {
  auto s = __builtin_amdgcn_permlane32_swap(__float_as_uint(a), __float_as_uint(b), false, false);
  return __uint_as_float(s[0]) + __uint_as_float(s[1]);
}
__device__ __forceinline__ float fold16(float a, float b) {
  auto s = __builtin_amdgcn_permlane16_swap(__float_as_uint(a), __float_as_uint(b), false, false);
  return __uint_as_float(s[0]) + __uint_as_float(s[1]);
}
__device__ __forceinline__ float xmax_rows(float x) {
  auto s = __builtin_amdgcn_permlane16_swap(__float_as_uint(x), __float_as_uint(x), false, false);
  x = fmaxf(__uint_as_float(s[0]), __uint_as_float(s[1]));
  auto t = __builtin_amdgcn_permlane32_swap(__float_as_uint(x), __float_as_uint(x), false, false);
  return fmaxf(__uint_as_float(t[0]), __uint_as_float(t[1]));
}
__device__ __forceinline__ unsigned cvt2(float lo, float hi) {
  typedef __bf16 bf16x2 __attribute__((ext_vector_type(2)));
  bf16x2 v = {(__bf16)lo, (__bf16)hi};
  return __builtin_bit_cast(unsigned, v);
}
__device__ __forceinline__ float rbf(float x) { return __uint_as_float(cvt2(x, x) << 16); }
__device__ __forceinline__ float bflo(unsigned v) { return __uint_as_float(v << 16); }
__device__ __forceinline__ float bfhi(unsigned v) { return __uint_as_float(v & 0xffff0000u); }
__device__ __forceinline__ u32x4 trfrag(unsigned a0, unsigned a1) {
  u32x2 lo = __builtin_bit_cast(u32x2, __builtin_amdgcn_ds_read_tr16_b64_v4i16((LAS s16x4*)(uintptr_t)a0));
  u32x2 hi = __builtin_bit_cast(u32x2, __builtin_amdgcn_ds_read_tr16_b64_v4i16((LAS s16x4*)(uintptr_t)a1));
  return u32x4{lo.x, lo.y, hi.x, hi.y};
}
__device__ __forceinline__ f32x4 mfma(u32x4 a, u32x4 b, f32x4 c) {
  return __builtin_amdgcn_mfma_f32_16x16x32_bf16(__builtin_bit_cast(bf16x8, a), __builtin_bit_cast(bf16x8, b), c, 0, 0, 0);
}
template <typename T> __device__ __forceinline__ T lds_ld(unsigned a) { return *(const LAS T*)(uintptr_t)a; }
template <typename T> __device__ __forceinline__ void lds_st(unsigned a, T v) { *(LAS T*)(uintptr_t)a = v; }
__device__ __forceinline__ unsigned nosent(float x) { return min(__float_as_uint(x), 0xfffffffeu); }
__device__ __forceinline__ unsigned umax4(u32x4 v) { return max(max(v.x, v.y), max(v.z, v.w)); }
__device__ __forceinline__ void flag_set(unsigned addr, unsigned val) {
  asm volatile("ds_write_b32 %0, %1" ::"v"(addr), "v"(val) : "memory");
}
__device__ __forceinline__ u32x4 flag_get4(unsigned addr) {
  u32x4 r;
  asm volatile("ds_read_b128 %0, %1\n\ts_waitcnt lgkmcnt(0)" : "=&v"(r) : "v"(addr) : "memory");
  return r;
}
template <int MASK> __device__ __forceinline__ void flag_wait(unsigned addr, unsigned lvl) {
#pragma unroll 1
  for (unsigned spin = 0; spin < (SPIN_CAP << 4); ++spin) {
    const u32x4 f = flag_get4(addr);
    unsigned m = ~0u;
    if (MASK & 1) m = min(m, f.x);
    if (MASK & 2) m = min(m, f.y);
    if (MASK & 4) m = min(m, f.z);
    if (MASK & 8) m = min(m, f.w);
    if ((unsigned)__builtin_amdgcn_readfirstlane(m) >= lvl) break;
  }
}
__device__ __forceinline__ void count_wait(unsigned addr, unsigned cnt) {
#pragma unroll 1
  for (unsigned spin = 0; spin < (SPIN_CAP << 4); ++spin) {
    unsigned f;
    asm volatile("ds_read_b32 %0, %1\n\ts_waitcnt lgkmcnt(0)" : "=&v"(f) : "v"(addr) : "memory");
    if (!__any(f < cnt)) break;
  }
}
__device__ __forceinline__ void loaders_wait(unsigned laddr, unsigned lvl) {
#pragma unroll 1
  for (unsigned spin = 0; spin < (SPIN_CAP << 4); ++spin) {
    unsigned f;
    asm volatile("ds_read_b32 %0, %1\n\ts_waitcnt lgkmcnt(0)" : "=&v"(f) : "v"(laddr) : "memory");
    if (!__any(f < lvl)) break;
  }
}

__device__ __forceinline__ unsigned f24(float v) { return (__float_as_uint(v) + 0x80u) >> 8; }
__device__ __forceinline__ f32x4 sel4(f32x4 c0, f32x4 c1, f32x4 c2, f32x4 c3, unsigned p4) {
  f32x4 c01 = (p4 & 1) ? c1 : c0, c23 = (p4 & 1) ? c3 : c2;
  asm volatile("" : "+v"(c01), "+v"(c23));
  return (p4 & 2) ? c23 : c01;
}
__device__ __forceinline__ unsigned gbad(u32x4 v, unsigned tl, unsigned th) { return (((v.x ^ tl) | (v.z ^ tl)) >> 24) | (v.y ^ th) | (v.w ^ th); }
__device__ __forceinline__ float gval(unsigned v) { return __uint_as_float(v << 8); }

extern "C" __global__ void __launch_bounds__(832, 4)
gated_mix_kernel(const u16* a_x, const u16* a_kc, const u16* a_vc, const u16* a_wq, const u16* a_wo, const u16* a_w1, const u16* a_b1,
                 const u16* a_w2, const u16* a_b2, const u16* a_gw, u16* a_out, unsigned char* a_ws) {
  __shared__ __attribute__((aligned(16))) unsigned char lds[LDS_BYTES];
  asm volatile("" ::"v"((LAS unsigned char*)lds) : "memory");
  const unsigned tid = threadIdx.x, lane = tid & 63, w = __builtin_amdgcn_readfirstlane(tid >> 6);
  u32x16 ka;
  u32x8 kb;
  asm volatile("s_load_dwordx16 %0, %2, 0x0\n\ts_load_dwordx8 %1, %2, 0x40" : "=&s"(ka), "=&s"(kb) : "s"(__builtin_amdgcn_kernarg_segment_ptr()));
  const unsigned bid = blockIdx.x, g = bid & 7, j = bid >> 3;
  const unsigned hi = lane >> 5, s32 = lane & 31, grp = lane >> 4, r16 = lane & 15, q4 = r16 >> 2, p4 = lane & 3;
  const size_t kvoff = ((size_t)g * SK + (SK - WIN) + j * 128) * 512;
  const unsigned jt = min(j, 27u), rb = jt / 7, cb = jt % 7;
  const unsigned hb = min(4 * g + rb, 27u);
  const unsigned fin = min(4 * g + j, 27u);
  const unsigned t1 = min(g * 32 + j, 223u);
  const unsigned zero = 0;
  const f32x4 z4 = {0.f, 0.f, 0.f, 0.f};
  const u32x4 zu = {0u, 0u, 0u, 0u};
  const unsigned c = w - 1, u = w - 5, lfa = F_L + (lane & 7) * 4;

  const unsigned qoff = ((j & 3) * 112 + u * 16) * 8192 + g * 1024 + (j >> 2) * 128;
  const unsigned hoff = ((t1 & 7) * 56 + u * 8) * 3584 + (t1 >> 3) * 128;
  const unsigned tv = (lane >> 3) * 8192 + (lane & 7) * 16, tw = (lane >> 3) * 3584 + (lane & 7) * 16;
  const size_t koff = kvoff + (u << 10);
  unsigned dl = K_OFF + u * 1024;
  unsigned dv = hi * 512 + ((s32 ^ hi ^ (u << 1)) << 4);
  asm volatile("s_waitcnt lgkmcnt(0)" : "+s"(ka), "+s"(kb) : "s"(qoff), "s"(hoff), "v"(tv), "v"(tw), "s"(koff), "s"(dl), "v"(dv));
#define KPTR(v, i) ((unsigned char*)(((u64)v[2 * (i) + 1] << 32) | v[2 * (i)]))
  const unsigned char *x = KPTR(ka, 0), *kc = KPTR(ka, 1), *vc = KPTR(ka, 2), *wq = KPTR(ka, 3), *wo = KPTR(ka, 4), *w1 = KPTR(ka, 5), *b1 = KPTR(ka, 6),
                      *w2 = KPTR(ka, 7), *b2 = KPTR(kb, 0), *gw = KPTR(kb, 1), *out = KPTR(kb, 2);
  unsigned char* ws = KPTR(kb, 3);
  const unsigned char* dsrc = kc + koff;
#define STEP() do { DMA1(dv, dsrc, dl); dsrc += 8192; dl += 8192; } while (0)
#define LOOP(cnt, body) _Pragma("unroll 1") for (unsigned i_ = (cnt); i_ != 0; --i_) { body; }
  u32x2 ep;
  u32x4 xf0, xf1, xf2, xf3;
  unsigned b2v, b1v;
  if (w >= 5) {
    if (u < 7) {
      asm volatile("s_mov_b32 m0, %3\n\ts_nop 0\n\tglobal_load_lds_dwordx4 %0, %2 nt\n\tglobal_load_lds_dwordx4 %1, %2 offset:1024 nt" ::"v"(tv), "v"(tv + 65536 - 1024),
                   "s"(wq + qoff), "s"(WQ_OFF + u * 2048) : "memory");
      DMA1(tw, w1 + hoff, W1_OFF + u * 1024);
    }
    LOOP(2, STEP());
    asm volatile("" : "=v"(ep), "=v"(xf0), "=v"(xf1), "=v"(xf2), "=v"(xf3), "=v"(b2v), "=v"(b1v));
  } else {
    asm volatile("" : "=v"(ep), "=v"(xf0), "=v"(xf1), "=v"(xf2), "=v"(xf3), "=v"(b2v), "=v"(b1v));
    const unsigned vx = w == 3 ? min(lane, 55u) * 16 : grp * 16 + (w == 0 ? (j & 3) * 224 : (t1 & 7) * 112);
    if (w == 0 || w == 2) LDX2(ep, zero, ws + WS_EPOCH, "sc1");
    if (w != 1 && w != 4) {
      LDX4(xf0, vx, x, "");
      LDX4(xf1, vx + (w == 0 ? 64 : w == 3 ? 0 : 48), w == 3 ? gw : x, "");
    }
    if (w == 4) {
      const u32x4 sent = {~0u, ~0u, ~0u, ~0u};
      ST4(grp * 8192 + r16 * 16, sent, ws + WS_IN1 + g * 32768 + j * 256, "sc1");
    }
    if (w == 0) {
      LDX4(xf2, vx, x, "offset:128");
      LDX4(xf3, vx, x, "offset:160");
      if (lane < 28) lds_st<unsigned>(lane * 4, lane == C_T / 4 ? 16u : 0u);
      if (lane < 8) lds_st<unsigned>(MX + lane * 4, ~0u);
      lds_st<u32x2>(PS + lane * 8, u32x2{~0u, ~0u});
      lds_st<u32x4>(RED + lane * 16, u32x4{~0u, ~0u, ~0u, ~0u});
      asm volatile("s_waitcnt lgkmcnt(0)" ::: "memory");
    }
  }
  asm volatile("s_barrier" ::: "memory");
  if (__builtin_expect_with_probability(w >= 5, 1, 0.99)) {
    WAITV(2);
    flag_set(F_L + u * 4, 1u);
    unsigned r0 = C_X, r1 = C_K, r2 = C_K, tk;
    const unsigned one = 1;
#define TICKET() asm volatile("s_mov_b64 exec, 1\n\tds_add_rtn_u32 %0, %1, %2\n\ts_mov_b64 exec, -1" : "=v"(tkv) : "v"(C_T), "v"(one) : "memory")
#define TAKE() do { asm volatile("s_waitcnt lgkmcnt(0)" : "+v"(tkv)::"memory"); tk = __builtin_amdgcn_readfirstlane(tkv); } while (0)
#define LANDED(r) asm volatile("s_mov_b64 exec, 1\n\tds_add_u32 %0, %1\n\ts_mov_b64 exec, -1" ::"v"(r), "v"(one) : "memory")
#define ROT(c) do { WAITV(2); LANDED(r0); r0 = r1; r1 = r2; r2 = (c); } while (0)
    unsigned tkv;
    TICKET();
    TAKE();
    const unsigned char* kp = kc + kvoff;
    const unsigned dk = hi * 512 + ((s32 ^ hi) << 4);
#pragma unroll 1
    while (tk < 64) {
      const unsigned n = tk;
      TICKET();
      ROT(C_K);
      DMA1(dk ^ ((n & 7) << 5), kp + n * 1024, K_OFF + n * 1024);
      TAKE();
    }
    const unsigned char* vp = vc + kvoff - 65536;
    const unsigned dw = hi * 512 + ((s32 ^ (hi << 1)) << 4);
#pragma unroll 1
    while (tk < 128) {
      const unsigned n = tk;
      TICKET();
      ROT(n < 112 ? C_V1 : C_V2);
      DMA1(dw ^ ((n & 3) << 6), vp + n * 1024, K_OFF + n * 1024);
      TAKE();
    }
    unsigned q0 = C_X, q1 = C_X, q2 = C_X;
#define ROT6(c) do { WAITV(5); LANDED(q0); q0 = q1; q1 = q2; q2 = r0; r0 = r1; r1 = r2; r2 = (c); } while (0)
#define TAKE6() do { TAKE(); tk &= 0xffffu; } while (0)
    tk &= 0xffffu;
#pragma unroll 1
    while (tk < 128) {
      const unsigned n = tk;
      TICKET();
      ROT6(n < 64 ? C_K : n < 112 ? C_V1 : C_V2);
      DMA1(n < 64 ? dk ^ ((n & 7) << 5) : dw ^ ((n & 3) << 6), (n < 64 ? kp : vp) + n * 1024, K_OFF + n * 1024);
      TAKE6();
    }
    if (j < 28 && tk < 152) {
      flag_wait<3>(F_R, 1u);
      const unsigned char* po = wo + (size_t)(g * 512 + rb * 128) * 896 + cb * 128;
      const unsigned char* p2 = w2 + (size_t)hb * (64 * 896) + cb * 128 - 16 * 7168;
      const unsigned wv = (lane >> 3) * 896 + (lane & 7) * 16;
#pragma unroll 1
      while (tk < 152) {
        const unsigned k = tk - 128;
        TICKET();
        ROT6(C_W);
        DMA1(wv, (k < 16 ? po : p2) + k * 7168, WO_OFF + k * 1024);
        TAKE6();
      }
    }
    WAITV(5); LANDED(q0);
    WAITV(4); LANDED(q1);
    WAITV(3); LANDED(q2);
    WAITV(2); LANDED(r0);
    WAITV(1); LANDED(r1);
    WAITV(0); LANDED(r2);
    return;
  }
  asm volatile("s_waitcnt vmcnt(0)" : "+v"(ep), "+v"(xf0), "+v"(xf1), "+v"(xf2), "+v"(xf3), "+v"(b1v)::"memory");
  const u64 e1 = (((u64)(unsigned)__builtin_amdgcn_readfirstlane(ep.y) << 32) | (unsigned)__builtin_amdgcn_readfirstlane(ep.x)) + 1ull;
  const unsigned e_lo = (unsigned)e1, e_hi = (unsigned)(e1 >> 32);
  const unsigned tl = e_lo << 24, th = (unsigned)(e1 >> 8) << 1;

  if (w == 0) {
    xf3 = hi ? xf3 : zu;
    loaders_wait(lfa, 1u);
    {
      const unsigned a = WQ_OFF + (8 * grp + q4) * 128 + p4 * 32;
      f32x4 acc[4];
#pragma unroll
      for (int mu = 0; mu < 4; ++mu) {
        acc[mu] = mfma(trfrag(a + mu * 8, a + mu * 8 + 512), xf0, z4);
        acc[mu] = mfma(trfrag(a + 4096 + mu * 8, a + 4096 + mu * 8 + 512), xf1, acc[mu]);
        acc[mu] = mfma(trfrag(a + 8192 + mu * 8, a + 8192 + mu * 8 + 512), xf2, acc[mu]);
        acc[mu] = mfma(trfrag(a + 10240 + mu * 8, a + 10240 + mu * 8 + 512), xf3, acc[mu]);
      }
      const f32x4 v = sel4(acc[0], acc[1], acc[2], acc[3], p4);
      const unsigned char* qo = ws + WS_QG + g * 16384 + (j & 3) * 4096 + (j >> 2) * 512;
      if (r16 < 4) {
        ST4(grp * 64 + p4 * 16, (u32x4{tl | f24(v.x), th, tl | f24(v.y), th}), qo, "sc1");
        ST4(grp * 64 + p4 * 16, (u32x4{tl | f24(v.z), th, tl | f24(v.w), th}), qo, "offset:256 sc1");
      }
      flag_set(F_R, 1u);
    }
    {
      const unsigned char* qgp = uptr(ws + WS_QG + g * 16384);
      const unsigned vq = grp * 512 + r16 * 16;
      u32x4 qa[16];
      __builtin_amdgcn_s_sleep(16);
#pragma unroll 1
      for (unsigned spin = 0; spin < SPIN_CAP; ++spin) {
#pragma unroll
        for (int i = 0; i < 4; ++i) {
          const unsigned vo_ = vq + i * 4096;
          LD4(qa[4 * i], vo_, qgp, "sc1"); LD4(qa[4 * i + 1], vo_, qgp, "offset:256 sc1"); LD4(qa[4 * i + 2], vo_, qgp, "offset:2048 sc1"); LD4(qa[4 * i + 3], vo_, qgp, "offset:2304 sc1");
        }
        if (lane == 0) asm volatile("ds_or_b32 %0, %1" ::"v"(C_T), "v"(0x10000u) : "memory");
        asm volatile("s_waitcnt vmcnt(0)" : "+v"(qa[0]), "+v"(qa[1]), "+v"(qa[2]), "+v"(qa[3]), "+v"(qa[4]), "+v"(qa[5]), "+v"(qa[6]), "+v"(qa[7]),
                     "+v"(qa[8]), "+v"(qa[9]), "+v"(qa[10]), "+v"(qa[11]), "+v"(qa[12]), "+v"(qa[13]), "+v"(qa[14]), "+v"(qa[15])::"memory");
        unsigned bad = 0;
#pragma unroll
        for (int i = 0; i < 16; ++i) bad |= gbad(qa[i], tl, th);
        if (!__any(bad != 0u)) break;
      }
#pragma unroll
      for (int h = 0; h < 2; ++h) {
        float s[4];
#pragma unroll
        for (int e = 0; e < 4; ++e) {
          const int r = 2 * h + (e >> 1), k = (e & 1) * 2;
          s[e] = (gval(qa[r][k]) + gval(qa[4 + r][k])) + (gval(qa[8 + r][k]) + gval(qa[12 + r][k]));
        }
        lds_st<u32x2>(QS + h * 576 + lane * 8, u32x2{cvt2(s[0], s[1]), cvt2(s[2], s[3])});
      }
      flag_set(F_Q, 1u);
    }
    const unsigned char* hp = ws + WS_HG + hb * 4096;
    u32x4 h0, h1, h2, h3;
    LDU(b2v, ((lane & 7) * 2 + hi) * 2, b2 + fin * 32, "");
    asm volatile("global_load_dword %0, %1, %2" : "=v"(b1v) : "v"(s32 * 4), "s"(b1 + hb * 128) : "memory");
    {
      flag_wait<15>(F_C, 2u);
      const f32x4 mm = lds_ld<f32x4>(MX + (lane & 1) * 16), ls = lds_ld<f32x4>(SUMS + (lane & 1) * 16);
      const u32x4 ml = {tl | f24(fmaxf(fmaxf(mm.x, mm.y), fmaxf(mm.z, mm.w))), th, tl | f24((ls.x + ls.y) + (ls.z + ls.w)), th};
      if (lane < 2) ST4(lane * 512, ml, ws + WS_ML + g * 1024 + j * 16, "sc1");
    }
    LD4(h0, lane * 16, hp, "sc1"); LD4(h1, lane * 16, hp, "offset:1024 sc1"); LD4(h2, lane * 16, hp, "offset:2048 sc1"); LD4(h3, lane * 16, hp, "offset:3072 sc1");
    asm volatile("s_waitcnt vmcnt(0)" : "+v"(h0), "+v"(h1), "+v"(h2), "+v"(h3), "+v"(b1v), "+v"(b2v)::"memory");
    const f32x2 gt = lds_ld<f32x2>(GATE);
    if (j < 28) {
      {
#pragma unroll 1
        for (unsigned spin = 0; spin < SPIN_CAP; ++spin) {
          if (!__any((gbad(h0, tl, th) | gbad(h1, tl, th) | gbad(h2, tl, th) | gbad(h3, tl, th)) != 0u) || g == 7) break;
          LD4(h0, lane * 16, hp, "sc1"); LD4(h1, lane * 16, hp, "offset:1024 sc1"); LD4(h2, lane * 16, hp, "offset:2048 sc1"); LD4(h3, lane * 16, hp, "offset:3072 sc1");
          asm volatile("s_waitcnt vmcnt(0)" : "+v"(h0), "+v"(h1), "+v"(h2), "+v"(h3)::"memory");
        }
        float a0 = (gval(h0.x) + gval(h1.x)) + (gval(h2.x) + gval(h3.x)), a1 = (gval(h0.z) + gval(h1.z)) + (gval(h2.z) + gval(h3.z));
        a0 = fold32(a0, a0); a1 = fold32(a1, a1);
        a0 = rbf(rbf(a0) + bflo(b1v)); a1 = rbf(rbf(a1) + bfhi(b1v));
        a0 = a0 > 0.f ? a0 : 0.f; a1 = a1 > 0.f ? a1 : 0.f;
        if (lane < 32) lds_st<unsigned>(HS + lane * 4, g < 7 ? cvt2(gt.y * rbf(a0 * a0), gt.y * rbf(a1 * a1)) : 0u);
        flag_set(F_Q + 8, 1u);
      }
      {
        const unsigned char* mlp = ws + WS_ML + (g * 2 + (rb >> 1)) * 512;
        u32x4 ml;
#pragma unroll 1
        for (unsigned spin = 0; spin < SPIN_CAP; ++spin) {
          LD4(ml, s32 * 16, mlp, "sc1");
          asm volatile("s_waitcnt vmcnt(0)" : "+v"(ml)::"memory");
          if (!__any(gbad(ml, tl, th) != 0u)) break;
          __builtin_amdgcn_s_sleep(4);
        }
        const float m0 = gval(ml.x);
        const float Mx = xmax_rows(row_max(m0));
        const float w0 = __builtin_amdgcn_exp2f(m0 - Mx);
        float L = row_sum(w0 * gval(ml.z));
        L = fold16(L, L);
        const float ww = w0 * (gt.x / L);
        if (lane < 32) lds_st<u16>(WSC + lane * 2, (u16)cvt2(ww, ww));
        flag_set(F_Q + 12, 1u);
      }
      const unsigned char* o2 = ws + WS_IN2 + cb * 16384 + (g * 4 + rb) * 512;
      const unsigned char* in2 = ws + WS_IN2 + (fin >> 2) * 16384 + (fin & 3) * 128;
      const unsigned ra2 = RED + s32 * 8, vo2 = (lane >> 3) * 512 + (lane & 7) * 16;
      const float ob = gt.y * bflo(b2v);
      asm volatile("" ::"s"(o2), "s"(in2), "v"(ra2), "v"(vo2), "v"(ob));
      u32x2 r0, r1, r2, r3;
#pragma unroll 1
      for (unsigned spin = 0; spin < (SPIN_CAP << 4); ++spin) {
        asm volatile("ds_read_b64 %0, %4\n\tds_read_b64 %1, %4 offset:256\n\tds_read_b64 %2, %4 offset:512\n\tds_read_b64 %3, %4 offset:768\n\ts_waitcnt lgkmcnt(0)"
                     : "=&v"(r0), "=&v"(r1), "=&v"(r2), "=&v"(r3) : "v"(ra2) : "memory");
        if (!__any(max(max(r0.x, r1.x), max(r2.x, r3.x)) == ~0u)) break;
      }
      const f32x2 r = (__builtin_bit_cast(f32x2, r0) + __builtin_bit_cast(f32x2, r1)) + (__builtin_bit_cast(f32x2, r2) + __builtin_bit_cast(f32x2, r3));
      const u32x4 o = {tl | f24(r.x), th, tl | f24(r.y), th};
      if (lane < 32) ST4(lane * 16, o, o2, "sc1");
      if (j < 4 && g < 7) {
        u32x4 v0, v1, v2, v3;
        __builtin_amdgcn_s_sleep(6);
#pragma unroll 1
        for (unsigned spin = 0; spin < SPIN_CAP; ++spin) {
          LD4(v0, vo2, in2, "sc1"); LD4(v1, vo2 + 4096, in2, "sc1"); LD4(v2, vo2 + 8192, in2, "sc1"); LD4(v3, vo2 + 12288, in2, "sc1");
          asm volatile("s_waitcnt vmcnt(0)" : "+v"(v0), "+v"(v1), "+v"(v2), "+v"(v3)::"memory");
          if (!__any((gbad(v0, tl, th) | gbad(v1, tl, th) | gbad(v2, tl, th) | gbad(v3, tl, th)) != 0u)) break;
        }
        const float a0 = (gval(v0.x) + gval(v1.x)) + (gval(v2.x) + gval(v3.x)), a1 = (gval(v0.z) + gval(v1.z)) + (gval(v2.z) + gval(v3.z));
        float tt = fold32(a0, a1);
        tt = fold16(tt, tt);
        tt += dpp<ROR8>(tt);
        tt += ob;
        const unsigned ob2 = cvt2(tt, tt);
        if ((lane & 24) == 0) STH(((lane & 7) * 2 + hi) * 2, ob2, out + fin * 32, "");
        if (bid == 0 && lane == 0) { const u32x2 e = {e_lo, e_hi}; ST2(zero, e, ws + WS_EPOCH, "sc1"); }
      }
    }
  } else {
    if (w == 2) {
      xf1 = grp ? xf1 : zu;
      loaders_wait(lfa, 1u);
      const unsigned a = W1_OFF + (8 * grp + q4) * 128 + p4 * 8;
      f32x4 acc[4];
#pragma unroll
      for (int t = 0; t < 4; ++t) {
        acc[t] = mfma(xf0, trfrag(a + t * 32, a + t * 32 + 512), z4);
        acc[t] = mfma(xf1, trfrag(a + t * 32 + 3072, a + t * 32 + 3584), acc[t]);
      }
      const float v = hi ? ((grp & 1) ? acc[3].x : acc[2].x) : ((grp & 1) ? acc[1].x : acc[0].x);
      const u32x2 gr = {tl | f24(v), th};
      if (g < 7) ST2(lane * 8, gr, ws + WS_HG + t1 * 512, "sc1");
      flag_set(F_R + 4, 1u);
    } else if (w == 3) {
      float z = 0.f;
#pragma unroll
      for (int i = 0; i < 4; ++i) {
        const unsigned pr = cvt2(bflo(xf0[i]) * bflo(xf1[i]), bfhi(xf0[i]) * bfhi(xf1[i]));
        z += bflo(pr); z += bfhi(pr);
      }
      z = lane < 56 ? z : 0.f;
      z = row_sum(z);
      z = fold16(z, z); z = fold32(z, z);
      z = rbf(z);
      const float gate = rbf(1.f / (1.f + __expf(-z)));
      if (lane == 0) lds_st<f32x2>(GATE, f32x2{gate, rbf(1.f - gate)});
    }
    count_wait(C_K, 64u);
    u32x4 kf[16];
    {
      const unsigned a = K_OFF + c * 16384 + r16 * 512 + ((grp ^ r16) << 4);
#pragma unroll
      for (int st = 0; st < 16; ++st) kf[st] = lds_ld<u32x4>((a ^ ((st & 3) << 6)) + ((st >> 2) & 1) * 256 + (st >> 3) * 8192);
    }
    const unsigned qaddr = QS + ((r16 >> 2) & 1) * 576 + grp * 16, hsel = ((lane >> 2) & 1) * 16, pa = PS + ((r16 >> 2) & 1) * 64 + grp * 16;
    const unsigned sg = (q4 | ((grp & 1) << 2)) << 1;
    const unsigned a0 = V_OFF + (4 * grp + q4) * 512 + (((8 * c + 2 * p4) ^ sg) << 4);
    const unsigned off = (r16 >> 2) * 16384 + grp * 32 + p4 * 8;
    const unsigned char* o1 = ws + WS_IN1 + (g * 4 + (c >> 1)) * 8192 + j * 256 + (c & 1) * 128;
    asm volatile("" ::"v"(qaddr), "s"(o1));
    flag_wait<1>(F_Q, 1u);
    f32x4 sa = z4, sb = z4;
    {
      u32x4 qf[8];
#pragma unroll
      for (int st = 0; st < 8; ++st) qf[st] = lds_ld<u32x4>(qaddr + st * 64);
      asm volatile("" : "+v"(qf[0]), "+v"(qf[1]), "+v"(qf[2]), "+v"(qf[3]), "+v"(qf[4]), "+v"(qf[5]), "+v"(qf[6]), "+v"(qf[7]));
#pragma unroll
      for (int st = 0; st < 8; ++st) {
        sa = mfma(kf[st], qf[st], sa);
        sb = mfma(kf[8 + st], qf[st], sb);
      }
    }
    constexpr float C2 = 0.0625f * 1.4426950408889634f;
#pragma unroll
    for (int i = 0; i < 4; i += 2) {
      const unsigned ua = cvt2(sa[i], sa[i + 1]), ub = cvt2(sb[i], sb[i + 1]);
      sa[i] = bflo(ua); sa[i + 1] = bfhi(ua); sb[i] = bflo(ub); sb[i + 1] = bfhi(ub);
    }
    const float m = xmax_rows(fmaxf(fmaxf(fmaxf(sa.x, sa.y), fmaxf(sa.z, sa.w)), fmaxf(fmaxf(sb.x, sb.y), fmaxf(sb.z, sb.w)))) * C2;
    if ((lane & 59) == 0) lds_st<unsigned>(MX + hsel + c * 4, f24(m) << 8);
    u32x4 mm;
#pragma unroll 1
    for (unsigned spin = 0; spin < (SPIN_CAP << 4); ++spin) {
      mm = flag_get4(MX + hsel);
      if (!__any(umax4(mm) == ~0u)) break;
    }
    const float M = fmaxf(fmaxf(__uint_as_float(mm.x), __uint_as_float(mm.y)), fmaxf(__uint_as_float(mm.z), __uint_as_float(mm.w)));
    float t[8];
#pragma unroll
    for (int i = 0; i < 4; ++i) { t[i] = __builtin_amdgcn_exp2f(fmaf(sa[i], C2, -M)); t[4 + i] = __builtin_amdgcn_exp2f(fmaf(sb[i], C2, -M)); }
    if ((r16 & 11) == 0) lds_st<u32x4>(pa + c * 128, u32x4{min(cvt2(t[0], t[1]), 0xfffffffeu), cvt2(t[2], t[3]), cvt2(t[4], t[5]), cvt2(t[6], t[7])});
    float l = ((t[0] + t[1]) + (t[2] + t[3])) + ((t[4] + t[5]) + (t[6] + t[7]));
    l = fold16(l, l); l = fold32(l, l);
    if ((lane & 59) == 0) lds_st<float>(SUMS + hsel + c * 4, l);
    flag_set(F_C + c * 4, 2u);
    f32x4 cc[4] = {z4, z4, z4, z4};
    u32x4 pf[4];
#pragma unroll 1
    for (unsigned spin = 0; spin < (SPIN_CAP << 4); ++spin) {
      asm volatile("ds_read_b128 %0, %4\n\tds_read_b128 %1, %4 offset:128\n\tds_read_b128 %2, %4 offset:256\n\tds_read_b128 %3, %4 offset:384\n\ts_waitcnt lgkmcnt(0)"
                   : "=&v"(pf[0]), "=&v"(pf[1]), "=&v"(pf[2]), "=&v"(pf[3]) : "v"(pa) : "memory");
      if (!__any(max(max(pf[0].x, pf[1].x), max(pf[2].x, pf[3].x)) == ~0u)) break;
    }
    count_wait(C_V1, 48u);
#pragma unroll
    for (int ks = 0; ks < 3; ++ks)
#pragma unroll
      for (int mu = 0; mu < 4; ++mu) cc[mu] = mfma(trfrag(a0 + ks * 16384 + mu * 8, a0 + ks * 16384 + 8192 + mu * 8), pf[ks], cc[mu]);
    asm volatile("" : "+v"(cc[0]), "+v"(cc[1]), "+v"(cc[2]), "+v"(cc[3]), "+v"(pf[3]) : "v"(off));
    count_wait(C_V2, 16u);
    {
      u32x4 vf[4];
#pragma unroll
      for (int mu = 0; mu < 4; ++mu) vf[mu] = trfrag(a0 + 49152 + mu * 8, a0 + 57344 + mu * 8);
      asm volatile("" : "+v"(vf[0]), "+v"(vf[1]), "+v"(vf[2]), "+v"(vf[3]));
#pragma unroll
      for (int mu = 0; mu < 4; ++mu) cc[mu] = mfma(vf[mu], pf[3], cc[mu]);
      const f32x4 cs = sel4(cc[0], cc[1], cc[2], cc[3], p4);
      const u32x2 o = {min(cvt2(cs.x, cs.y), 0xfffffffeu), min(cvt2(cs.z, cs.w), 0xfffffffeu)};
      if (r16 < 8) ST2(off, o, o1, "sc1");
    }
    if (j < 28) {
      count_wait(C_W, 24u);
      f32x4 acc[4];
      u32x4 wf[4];
      const unsigned ra = WO_OFF + (32 * c + 4 * grp + q4) * 128 + p4 * 8;
#pragma unroll
      for (int t2 = 0; t2 < 4; ++t2) { wf[t2] = trfrag(ra + t2 * 32, ra + t2 * 32 + 2048); acc[t2] = z4; }
      if (c == 1 || c == 2) {
        flag_wait<4>(F_Q, 1u);
        const unsigned rh = W2_OFF + (32 * (c - 1) + 8 * grp + q4) * 128 + p4 * 8;
        const u32x4 hf = lds_ld<u32x4>(HS + (c - 1) * 64 + grp * 16);
#pragma unroll
        for (int t2 = 0; t2 < 4; ++t2) acc[t2] = mfma(trfrag(rh + t2 * 32, rh + t2 * 32 + 512), hf, acc[t2]);
      }
      flag_wait<8>(F_Q, 1u);
      const u32x4 wsf = lds_ld<u32x4>(WSC + grp * 16);
      const unsigned char* in1 = ws + WS_IN1 + (g * 4 + rb) * 8192 + c * 64;
      const unsigned vo = (lane >> 2) * 256 + p4 * 16;
      const unsigned sw = STG + c * 2048 + (lane >> 2) * 64 + ((p4 ^ (hi << 1)) << 4);
      const unsigned sr = STG + c * 2048 + (8 * grp + q4) * 64 + (grp & 1) * 32 + p4 * 8;
      const unsigned rw = RED + c * 256 + grp * 16;
      asm volatile("" : "+v"(wf[0]), "+v"(wf[1]), "+v"(wf[2]), "+v"(wf[3]), "+v"(acc[0]), "+v"(acc[1]), "+v"(acc[2]), "+v"(acc[3]) : "v"(wsf), "v"(vo), "v"(sw), "v"(sr), "v"(rw), "s"(in1));
      u32x4 v0, v1;
#pragma unroll 1
      for (unsigned spin = 0; spin < SPIN_CAP; ++spin) {
        LD4(v0, vo, in1, "sc1");
        LD4(v1, vo + 4096, in1, "sc1");
        asm volatile("s_waitcnt vmcnt(0)" : "+v"(v0), "+v"(v1)::"memory");
        if (!__any(max(umax4(v0), umax4(v1)) == ~0u)) break;
      }
      lds_st<u32x4>(sw, v0);
      lds_st<u32x4>(sw + 1024, v1);
      const f32x4 oa = mfma(trfrag(sr, sr + 256), wsf, z4), ob = mfma(trfrag(sr ^ 32, (sr ^ 32) + 256), wsf, z4);
      const u32x4 of = {cvt2(oa.x, oa.y), cvt2(oa.z, oa.w), cvt2(ob.x, ob.y), cvt2(ob.z, ob.w)};
#pragma unroll
      for (int t2 = 0; t2 < 4; ++t2) acc[t2] = mfma(wf[t2], of, acc[t2]);
      if (r16 == 0) {
#pragma unroll
        for (int t2 = 0; t2 < 4; ++t2) lds_st<u32x4>(rw + t2 * 64, u32x4{nosent(acc[t2].x), __float_as_uint(acc[t2].y), nosent(acc[t2].z), __float_as_uint(acc[t2].w)});
      }
    }
  }
}

extern "C" void kernel_launch(void* const* d_in, const int* in_sizes, int n_in, void* d_out, int out_size, void* d_ws,
                              size_t ws_size, hipStream_t stream) {
  gated_mix_kernel<<<256, 832, 0, stream>>>((const u16*)d_in[0], (const u16*)d_in[1], (const u16*)d_in[2], (const u16*)d_in[3],
                                            (const u16*)d_in[4], (const u16*)d_in[6], (const u16*)d_in[7], (const u16*)d_in[8],
                                            (const u16*)d_in[9], (const u16*)d_in[10], (u16*)d_out, (unsigned char*)d_ws);
}
```
